```python
import torch

def _softmax(x, axis=-1):
    return torch.softmax(x, dim=axis)
def _gelu(x):
    return torch.nn.functional.gelu(x, approximate="tanh")
def _silu(x):
    return torch.nn.functional.silu(x)
def _sigmoid(x):
    return torch.sigmoid(x)
def _layernorm(x, axis=-1, eps=1e-5):
    m = x.mean(dim=axis, keepdim=True)
    v = x.var(dim=axis, keepdim=True, unbiased=False)
    return (x - m) / torch.sqrt(v + eps)
def _scatter_add(tgt, idx, src, axis=0):
    return tgt.clone().index_add_(axis, idx, src)
def _segment_sum(src, seg, n):
    out = torch.zeros((n,) + src.shape[1:], dtype=src.dtype)
    return out.index_add_(0, seg, src)
def _pad(x, pw):
    flat = tuple(v for pair in reversed(pw) for v in pair)
    return torch.nn.functional.pad(x, flat)

kernel_name = 'interleave_layers__ssm_conv_s4__geglu_ffn__prompt'

def setup_inputs():

    _w = (lambda a: (lambda t: t.to(torch.bfloat16) if t.is_floating_point() else t)(torch.as_tensor(a)))
    def _A_setup():
        B, L, D, N = 1, 2048, 512, 128
        A_re = -(lambda _x: torch.exp(torch.as_tensor(_x)))((lambda *s: torch.randn(*s, dtype=torch.bfloat16) * 1.6)(D, N))
        A_im = torch.pi * (lambda *s: torch.randn(*s, dtype=torch.bfloat16) * 0.2)(D, N)
        B_re = (lambda *s: torch.randn(*s, dtype=torch.bfloat16) * 0.2)(D, N)
        B_im = (lambda *s: torch.randn(*s, dtype=torch.bfloat16) * 0.2)(D, N)
        C_re = (lambda *s: torch.randn(*s, dtype=torch.bfloat16) * 0.2)(D, N)
        C_im = (lambda *s: torch.randn(*s, dtype=torch.bfloat16) * 0.2)(D, N)
        log_dt = (lambda *s: torch.randn(*s, dtype=torch.bfloat16) * 0.2)(D) * 0.5 - 4.0
        Dskip = (lambda *s: torch.randn(*s, dtype=torch.bfloat16) * 0.2)(D)
        return {
            "u": _w((lambda *s: torch.randn(*s, dtype=torch.bfloat16) * 0.05)(B, L, D)),
            "A_re": _w(A_re),
            "A_im": _w(A_im),
            "B_re": _w(B_re),
            "B_im": _w(B_im),
            "C_re": _w(C_re),
            "C_im": _w(C_im),
            "log_dt": _w(log_dt),
            "Dskip": _w(Dskip),
        }
    _A_in = _A_setup()

    def _B_setup():
        B, S, D, F = 1, 2048, 512, 1792
        return {
            "x": _w((lambda *s: torch.randn(*s, dtype=torch.bfloat16) * 0.05)(B, S, D)),
            "w_gate": _w((lambda *s: torch.randn(*s, dtype=torch.bfloat16) * 0.05)(D, F)),
            "w_up": _w((lambda *s: torch.randn(*s, dtype=torch.bfloat16) * 0.05)(D, F)),
            "w_down": _w((lambda *s: torch.randn(*s, dtype=torch.bfloat16) * 0.05)(F, D)),
        }
    _B_in = _B_setup()

    return {
        "A_u": _A_in["u"],
        "A_A_re": _A_in["A_re"],
        "A_A_im": _A_in["A_im"],
        "A_B_re": _A_in["B_re"],
        "A_B_im": _A_in["B_im"],
        "A_C_re": _A_in["C_re"],
        "A_C_im": _A_in["C_im"],
        "A_log_dt": _A_in["log_dt"],
        "A_Dskip": _A_in["Dskip"],
        "B_x": _B_in["x"],
        "B_w_gate": _B_in["w_gate"],
        "B_w_up": _B_in["w_up"],
        "B_w_down": _B_in["w_down"],
    }

def reference(A_u, A_A_re, A_A_im, A_B_re, A_B_im, A_C_re, A_C_im, A_log_dt, A_Dskip, B_x, B_w_gate, B_w_up, B_w_down):
    def _A_ref(u, A_re, A_im, B_re, B_im, C_re, C_im, log_dt, Dskip):
        Bsz, L, D = u.shape
        dt = torch.exp(log_dt)[:, None]
        A = A_re + 1j * A_im
        Bc = B_re + 1j * B_im
        Cc = C_re + 1j * C_im
        logdA = dt * A
        dB = dt * Bc
        l = (lambda x, dt: x.to(dt))(torch.arange(L), A_re.dtype)
        P = torch.exp(logdA[:, :, None] * l[None, None, :])
        K = torch.einsum("dn,dnl,dn->dl", Cc, P, dB).real
        L2 = 2 * L
        Kp = _pad(K, ((0, 0), (0, L)))
        up = (lambda x, *d: x.permute(*d))(u, 0, 2, 1)
        up = _pad(up, ((0, 0), (0, 0), (0, L)))
        Kf = (lambda x, axes=(-2,-1): torch.fft.rfft2(x.float(), dim=axes))(Kp[:, None, :], axes=(-2, -1))
        Uf = (lambda x, axes=(-2,-1): torch.fft.rfft2(x.float(), dim=axes))(up[:, :, None, :], axes=(-2, -1))
        Yf = Uf * Kf[None, :, :, :]
        y = (lambda x, s, axes=(-2,-1): torch.fft.irfft2(x, s=s, dim=axes).to(torch.bfloat16))(Yf, (1, L2), axes=(-2, -1))[:, :, 0, :L]
        y = (lambda x, *d: x.permute(*d))(y, 0, 2, 1) + u * Dskip[None, None, :]
        return y

    def _B_ref(x, w_gate, w_up, w_down):
        return (_gelu(x @ w_gate) * (x @ w_up)) @ w_down

    x = A_u
    x = x + _A_ref(_layernorm(x, axis=-1), A_A_re, A_A_im, A_B_re, A_B_im, A_C_re, A_C_im, A_log_dt, A_Dskip)
    x = x + _B_ref(_layernorm(x, axis=-1), B_w_gate, B_w_up, B_w_down)
    x = x + _A_ref(_layernorm(x, axis=-1), A_A_re, A_A_im, A_B_re, A_B_im, A_C_re, A_C_im, A_log_dt, A_Dskip)
    x = x + _B_ref(_layernorm(x, axis=-1), B_w_gate, B_w_up, B_w_down)
    return x
```

```cpp
#include <hip/hip_runtime.h>
#include <hip/hip_bf16.h>
#include <stdint.h>

typedef unsigned short u16;
typedef unsigned int u32;
using bf16x8 = __attribute__((ext_vector_type(8))) short;
using bf16x4 = __attribute__((ext_vector_type(4))) short;
using f32x16 = __attribute__((ext_vector_type(16))) float;
#define DI __device__ __forceinline__
#define MFMA32(a, b, c) __builtin_amdgcn_mfma_f32_32x32x16_bf16((a), (b), (c), 0, 0, 0)

constexpr int SL = 2048;
constexpr int DM = 512;
constexpr int NST = 128;
constexpr int FF = 1792;
constexpr int NGU = 2 * FF;

constexpr size_t KCOPY = 4160;
constexpr size_t KIMG = 4 * KCOPY;
constexpr size_t WS_KIMG = 0;
constexpr size_t WS_WGU = WS_KIMG + (size_t)DM * 2 * KIMG;
constexpr size_t WS_WD = WS_WGU + (size_t)NGU * DM * 2;
constexpr size_t WS_CS = WS_WD + (size_t)DM * FF * 2;
constexpr size_t WS_ST = WS_CS + (size_t)NGU * 4;
constexpr size_t WS_X1 = WS_ST + (size_t)SL * 8 * 8;
constexpr size_t WS_X2 = WS_X1 + (size_t)SL * DM * 2;
constexpr size_t WS_H = WS_X2 + (size_t)SL * DM * 2;
constexpr size_t WS_END = WS_H + (size_t)SL * FF * 2;

DI float bf2f(u16 v) { return __uint_as_float((u32)v << 16); }
DI u16 f2bf(float f) {
  u32 u = __float_as_uint(f);
  if ((u & 0x7fffffffu) > 0x7f800000u) return (u16)((u >> 16) | 0x40u);
  u += 0x7fffu + ((u >> 16) & 1u);
  return (u16)(u >> 16);
}
DI float rbf(float f) { return bf2f(f2bf(f)); }
DI float wave_sum(float v) {
#pragma unroll
  for (int o = 32; o >= 1; o >>= 1) v += __shfl_xor(v, o, 64);
  return v;
}
struct cf { float r, i; };
DI cf cmul(cf a, cf b) { cf c; c.r = a.r * b.r - a.i * b.i; c.i = a.r * b.i + a.i * b.r; return c; }

DI void ln_scalars(float mean, float m2, float& m_bf, float& s_bf) {
  m_bf = rbf(mean);
  float v_bf = rbf(m2 * (1.0f / DM));
  s_bf = rbf(sqrtf(rbf(v_bf + 1e-5f)));
}

constexpr int PREP_T = 320;
constexpr int NB_KGEN = DM;
constexpr int NB_STAT = SL / 80 + 1;
constexpr int NB_TRW = 3 * (DM / 64) * (FF / 64);
constexpr int NB_CS = NGU / 64;
constexpr int NB_PREP = NB_KGEN + NB_STAT + NB_TRW + NB_CS;

__global__ __launch_bounds__(PREP_T) void prep_kernel(
    const u16* __restrict__ x0, const u16* __restrict__ A_re, const u16* __restrict__ A_im,
    const u16* __restrict__ B_re, const u16* __restrict__ B_im, const u16* __restrict__ C_re,
    const u16* __restrict__ C_im, const u16* __restrict__ log_dt, const u16* __restrict__ wg,
    const u16* __restrict__ wu, const u16* __restrict__ wd, char* __restrict__ ws) {
  __shared__ __attribute__((aligned(16))) float2 sR[4][64][16];
  __shared__ __attribute__((aligned(16))) float2 sQ[5][64][8];
  __shared__ float sK[648];
  const int t = threadIdx.x;
  int b = blockIdx.x;
  if (b < NB_KGEN) {
    const int d = b;
    const float dt = rbf(expf(bf2f(log_dt[d])));
    if (t == 0) sK[640] = 0.f;
    const int tile = t >> 4, ns = t & 15, blk = tile >> 2, qq = tile & 3;
    float acc[8][4];
#pragma unroll
    for (int p = 0; p < 8; ++p)
#pragma unroll
      for (int q = 0; q < 4; ++q) acc[p][q] = 0.f;
    for (int r = 0; r < 2; ++r) {
      if (t < 128) {
        const int nn = t & 63, n = r * 64 + nn;
        const float lr = dt * bf2f(A_re[d * NST + n]);
        const float li = dt * bf2f(A_im[d * NST + n]);
        if (t < 64) {
          cf E[7];
#pragma unroll
          for (int k = 0; k < 7; ++k) {
            float sc = (float)(1 << k), er = expf(lr * sc), sn, cs;
            sincosf(li * sc, &sn, &cs);
            E[k].r = er * cs; E[k].i = er * sn;
          }
#pragma unroll
          for (int tau = 0; tau < 4; ++tau) {
            cf rr[16];
            rr[0].r = 1.f; rr[0].i = 0.f;
            rr[1] = E[tau]; rr[2] = E[tau + 1]; rr[3] = cmul(rr[2], E[tau]);
            rr[4] = E[tau + 2]; rr[5] = cmul(rr[4], E[tau]); rr[6] = cmul(rr[4], E[tau + 1]);
            rr[7] = cmul(rr[6], E[tau]); rr[8] = E[tau + 3];
#pragma unroll
            for (int q = 1; q < 8; ++q) rr[8 + q] = cmul(rr[8], rr[q]);
#pragma unroll
            for (int q = 0; q < 16; ++q) sR[tau][nn][q] = make_float2(rr[q].r, rr[q].i);
          }
        } else {
          const float dBr = dt * bf2f(B_re[d * NST + n]), dBi = dt * bf2f(B_im[d * NST + n]);
          const float Cr = bf2f(C_re[d * NST + n]), Ci = bf2f(C_im[d * NST + n]);
          cf W; W.r = Cr * dBr - Ci * dBi; W.i = Cr * dBi + Ci * dBr;
          cf E[12];
#pragma unroll
          for (int k = 4; k < 12; ++k) {
            float sc = (float)(1 << k), er = expf(lr * sc), sn, cs;
            sincosf(li * sc, &sn, &cs);
            E[k].r = er * cs; E[k].i = er * sn;
          }
#pragma unroll
          for (int bb = 0; bb < 5; ++bb) {
            const int tau = bb == 0 ? 0 : bb - 1;
            cf base = bb == 0 ? W : cmul(W, E[7 + tau]);
            cf e0 = E[tau + 4], e1 = E[tau + 5], e2 = E[tau + 6];
            cf q0 = base, q1 = cmul(base, e0), q2 = cmul(base, e1), q3 = cmul(q2, e0);
            cf q4 = cmul(base, e2), q5 = cmul(q4, e0), q6 = cmul(q4, e1), q7 = cmul(q6, e0);
            sQ[bb][nn][0] = make_float2(q0.r, q0.i); sQ[bb][nn][1] = make_float2(q1.r, q1.i);
            sQ[bb][nn][2] = make_float2(q2.r, q2.i); sQ[bb][nn][3] = make_float2(q3.r, q3.i);
            sQ[bb][nn][4] = make_float2(q4.r, q4.i); sQ[bb][nn][5] = make_float2(q5.r, q5.i);
            sQ[bb][nn][6] = make_float2(q6.r, q6.i); sQ[bb][nn][7] = make_float2(q7.r, q7.i);
          }
          cf last = cmul(W, E[11]);
          atomicAdd(&sK[640], last.r);
        }
      }
      __syncthreads();
      {
        const int tau = blk == 0 ? 0 : blk - 1;
#pragma unroll
        for (int i = 0; i < 4; ++i) {
          const int nn = ns * 4 + i;
          float2 Q[8], R[4];
#pragma unroll
          for (int p = 0; p < 8; ++p) Q[p] = sQ[blk][nn][p];
#pragma unroll
          for (int q = 0; q < 4; ++q) R[q] = sR[tau][nn][qq * 4 + q];
#pragma unroll
          for (int p = 0; p < 8; ++p)
#pragma unroll
            for (int q = 0; q < 4; ++q) acc[p][q] = fmaf(Q[p].x, R[q].x, fmaf(-Q[p].y, R[q].y, acc[p][q]));
        }
      }
      __syncthreads();
    }
#pragma unroll
    for (int p = 0; p < 8; ++p)
#pragma unroll
      for (int q = 0; q < 4; ++q) {
        float v = acc[p][q];
        v += __shfl_xor(v, 1, 64); v += __shfl_xor(v, 2, 64);
        v += __shfl_xor(v, 4, 64); v += __shfl_xor(v, 8, 64);
        if (ns == 0) sK[blk * 128 + p * 16 + qq * 4 + q] = v;
      }
    __syncthreads();
    u32* img = (u32*)(ws + WS_KIMG + (size_t)d * 2 * KIMG);
    for (int e = t; e < 2 * 4 * 1040; e += PREP_T) {
      const int part = e / 4160, rem = e % 4160, c = rem / 1040, j = rem % 1040;
      u32 out = 0;
#pragma unroll
      for (int h = 0; h < 2; ++h) {
        const int i = 2 * j + h + c;
        const int l = 2047 - i;
        float kv = 0.f;
        if (l >= 0) {
          const float lf = rbf((float)l);
          const int v = (int)lf;
          int idx;
          if (v < 256) idx = v;
          else if (v < 512) idx = 256 + ((v - 256) >> 1);
          else if (v < 1024) idx = 384 + ((v - 512) >> 2);
          else if (v < 2048) idx = 512 + ((v - 1024) >> 3);
          else idx = 640;
          kv = sK[idx];
        }
        const u16 hi = f2bf(kv);
        const u16 o16 = part == 0 ? hi : f2bf(kv - bf2f(hi));
        out |= (u32)o16 << (16 * h);
      }
      img[e] = out;
    }
    return;
  }
  b -= NB_KGEN;
  if (b < NB_STAT) {
    const int w = t >> 6, lane = t & 63;
    float2* st = (float2*)(ws + WS_ST);
    for (int i = 0; i < 16; ++i) {
      const int row = b * 80 + w * 16 + i;
      if (row >= SL) break;
      const uint4 v = *(const uint4*)(x0 + (size_t)row * DM + lane * 8);
      float f[8];
      const u32 vv[4] = {v.x, v.y, v.z, v.w};
#pragma unroll
      for (int k = 0; k < 4; ++k) { f[2 * k] = bf2f(vv[k] & 0xffff); f[2 * k + 1] = bf2f(vv[k] >> 16); }
      float s = 0.f;
#pragma unroll
      for (int k = 0; k < 8; ++k) s += f[k];
      const float mean = wave_sum(s) * (1.0f / DM);
      float q = 0.f;
#pragma unroll
      for (int k = 0; k < 8; ++k) { float dd = f[k] - mean; q += dd * dd; }
      q = wave_sum(q);
      if (lane == 0) st[row] = make_float2(mean, q);
    }
    return;
  }
  b -= NB_STAT;
  if (b < NB_TRW) {
    u16(*tl)[66] = (u16(*)[66])&sR[0][0][0];
    const int which = b / 224, tb = b % 224;
    const u16* src; u16* dst; int ld_src, ld_dst, r0, c0, dst_row0;
    if (which < 2) {
      src = which == 0 ? wg : wu; ld_src = FF;
      r0 = (tb / 28) * 64; c0 = (tb % 28) * 64;
      dst = (u16*)(ws + WS_WGU); ld_dst = DM;
      dst_row0 = (c0 / 64) * 128 + which * 64;
    } else {
      src = wd; ld_src = DM;
      r0 = (tb / 8) * 64; c0 = (tb % 8) * 64;
      dst = (u16*)(ws + WS_WD); ld_dst = FF;
      dst_row0 = c0;
    }
    if (t < 256) {
      const int row = t >> 2, cq = (t & 3) * 16;
      const uint4 v0 = *(const uint4*)(src + (size_t)(r0 + row) * ld_src + c0 + cq);
      const uint4 v1 = *(const uint4*)(src + (size_t)(r0 + row) * ld_src + c0 + cq + 8);
      const u32 vv[8] = {v0.x, v0.y, v0.z, v0.w, v1.x, v1.y, v1.z, v1.w};
#pragma unroll
      for (int k = 0; k < 8; ++k) { tl[row][cq + 2 * k] = vv[k] & 0xffff; tl[row][cq + 2 * k + 1] = vv[k] >> 16; }
    }
    __syncthreads();
    if (t < 256) {
      const int oc = t >> 2, rq = (t & 3) * 16;
      u32 o[8];
#pragma unroll
      for (int k = 0; k < 8; ++k) o[k] = (u32)tl[rq + 2 * k][oc] | ((u32)tl[rq + 2 * k + 1][oc] << 16);
      uint4* dp = (uint4*)(dst + (size_t)(dst_row0 + oc) * ld_dst + r0 + rq);
      dp[0] = make_uint4(o[0], o[1], o[2], o[3]);
      dp[1] = make_uint4(o[4], o[5], o[6], o[7]);
    }
    return;
  }
  b -= NB_TRW;
  {
    float* red = (float*)&sQ[0][0][0];
    const int which = b / 28, f0 = (b % 28) * 64;
    const u16* src = which == 0 ? wg : wu;
    if (t < 256) {
      const int c = t & 63, g = t >> 6;
      float s = 0.f;
      for (int r = g * 128; r < g * 128 + 128; ++r) s += bf2f(src[(size_t)r * FF + f0 + c]);
      red[t] = s;
    }
    __syncthreads();
    if (t < 64) {
      float* cs = (float*)(ws + WS_CS);
      cs[(f0 / 64) * 128 + which * 64 + t] = red[t] + red[t + 64] + red[t + 128] + red[t + 192];
    }
  }
}

constexpr int UIMG = 96 * 64;
constexpr int CONV_LDS = 4 * KIMG + 2 * UIMG + SL * 8 + 2 * 32 * 65 * 4;

__global__ __launch_bounds__(256) void conv_kernel(const u16* __restrict__ x, const float2* __restrict__ stats,
                                                   int P, const char* __restrict__ kimg,
                                                   const u16* __restrict__ dskip, u16* __restrict__ xout) {
  extern __shared__ __attribute__((aligned(16))) char smem[];
  char* sKI = smem;
  char* sU = smem + 4 * KIMG;
  float2* sSt = (float2*)(smem + 4 * KIMG + 2 * UIMG);
  float* sY = (float*)(smem + 4 * KIMG + 2 * UIMG + SL * 8);
  const int t = threadIdx.x, w = t >> 6, lane = t & 63;
  const int ch0 = blockIdx.x * 2;
  {
    const char* src = kimg + (size_t)(ch0 + (w >> 1)) * 2 * KIMG + (size_t)(w & 1) * KIMG;
    char* dst = sKI + w * KIMG;
    for (int i = 0; i < 17; ++i) {
      const int off = i * 1024 + lane * 16;
      if (i < 16 || lane < 16)
        __builtin_amdgcn_global_load_lds((const u32*)(src + off), (u32*)(dst + i * 1024 + lane * 16), 16, 0, 0);
    }
  }
  for (int i = t; i < 2 * 32 * 16; i += 256) {
    const int c = i / 512, r = i % 512;
    *(u32*)(sU + c * UIMG + r * 4) = 0u;
  }
  for (int i = 0; i < 8; ++i) {
    const int row = t + 256 * i;
    float mean = 0.f, m2 = 0.f;
    if (P == 1) { float2 s = stats[row]; mean = s.x; m2 = s.y; }
    else {
      float mt[8]; float q = 0.f;
      for (int p = 0; p < 8; ++p) { float2 s = stats[row * 8 + p]; mt[p] = s.x; q += s.y; mean += s.x; }
      mean *= 0.125f;
      for (int p = 0; p < 8; ++p) { float dd = mt[p] - mean; q += 64.f * dd * dd; }
      m2 = q;
    }
    float m_bf, s_bf; ln_scalars(mean, m2, m_bf, s_bf);
    sSt[row] = make_float2(m_bf, s_bf);
  }
  __syncthreads();
  for (int i = 0; i < 8; ++i) {
    const int l = t + 256 * i;
    const u32 xv = *(const u32*)(x + (size_t)l * DM + ch0);
    const float2 st = sSt[l];
    const int row = (l >> 5) + 32, a = l & 31;
    const int off = row * 64 + (((a >> 3) ^ ((row >> 2) & 3)) << 4) + (a & 7) * 2;
#pragma unroll
    for (int c = 0; c < 2; ++c) {
      const float xf = bf2f(c ? (xv >> 16) : (xv & 0xffff));
      const float u = rbf(rbf(xf - st.x) / st.y);
      *(u16*)(sU + c * UIMG + off) = f2bf(u);
    }
  }
  asm volatile("s_waitcnt vmcnt(0)" ::: "memory");
  __syncthreads();
  const int m = lane & 31, kg = lane >> 5;
  const int chl = w >> 1;
  const int s0 = 2047 + 8 * kg - m, c0 = s0 & 3;
  const char* abase = sKI + w * KIMG + c0 * KCOPY + (s0 - c0) * 2;
  const char* ubase = sU + chl * UIMG;
  f32x16 acc0, acc1;
#pragma unroll
  for (int i = 0; i < 16; ++i) { acc0[i] = 0.f; acc1[i] = 0.f; }
  auto afrag = [&](int dl, int ks) -> bf16x8 {
    const char* p = abase - 64 * dl + 32 * ks;
    const bf16x4 lo = *(const bf16x4*)p, hi = *(const bf16x4*)(p + 8);
    bf16x8 r; r[0] = lo[0]; r[1] = lo[1]; r[2] = lo[2]; r[3] = lo[3]; r[4] = hi[0]; r[5] = hi[1]; r[6] = hi[2]; r[7] = hi[3];
    return r;
  };
  auto ufrag = [&](int w0, int ks) -> bf16x8 {
    const int row = w0 + m + 32;
    return *(const bf16x8*)(ubase + row * 64 + (((2 * ks + kg) ^ ((row >> 2) & 3)) << 4));
  };
#pragma unroll 2
  for (int dl = 0; dl < 32; ++dl) {
#pragma unroll
    for (int ks = 0; ks < 2; ++ks) {
      const bf16x8 a = afrag(dl, ks);
      acc1 = MFMA32(a, ufrag(32 - dl, ks), acc1);
      acc0 = MFMA32(a, ufrag(-dl, ks), acc0);
    }
  }
#pragma unroll 2
  for (int dl = 32; dl < 64; ++dl) {
#pragma unroll
    for (int ks = 0; ks < 2; ++ks) {
      const bf16x8 a = afrag(dl, ks);
      acc1 = MFMA32(a, ufrag(32 - dl, ks), acc1);
    }
  }
  float* yb = sY + chl * 32 * 65;
  if ((w & 1) == 0) {
#pragma unroll
    for (int i = 0; i < 16; ++i) {
      const int a = (i & 3) + 8 * (i >> 2) + 4 * kg;
      yb[a * 65 + m] = acc0[i];
      yb[a * 65 + 32 + m] = acc1[i];
    }
  }
  __syncthreads();
  if ((w & 1) == 1) {
#pragma unroll
    for (int i = 0; i < 16; ++i) {
      const int a = (i & 3) + 8 * (i >> 2) + 4 * kg;
      yb[a * 65 + m] += acc0[i];
      yb[a * 65 + 32 + m] += acc1[i];
    }
  }
  __syncthreads();
  const float dk0 = bf2f(dskip[ch0]), dk1 = bf2f(dskip[ch0 + 1]);
  for (int i = 0; i < 8; ++i) {
    const int l = t + 256 * i;
    const int a = l & 31, c = l >> 5;
    const u32 xv = *(const u32*)(x + (size_t)l * DM + ch0);
    const int row = c + 32;
    const int off = row * 64 + (((a >> 3) ^ ((row >> 2) & 3)) << 4) + (a & 7) * 2;
    u32 out = 0;
#pragma unroll
    for (int k = 0; k < 2; ++k) {
      const float y = rbf(sY[k * 32 * 65 + a * 65 + c]);
      const float u = bf2f(*(const u16*)(sU + k * UIMG + off));
      const float sk = rbf(u * (k ? dk1 : dk0));
      const float y2 = rbf(y + sk);
      const float xf = bf2f(k ? (xv >> 16) : (xv & 0xffff));
      out |= (u32)f2bf(xf + y2) << (16 * k);
    }
    *(u32*)(xout + (size_t)l * DM + ch0) = out;
  }
}

DI int swz_off(int row, int chunk) { return row * 128 + ((chunk ^ ((row >> 1) & 7)) << 4); }

template <int ROWS, int NT>
DI void stage_tile(const u16* g, int ld, char* lds, int t) {
  constexpr int N = ROWS * 8 / NT;
  const int w = t >> 6, lane = t & 63;
#pragma unroll
  for (int i = 0; i < N; ++i) {
    const int cidx = (i * (NT / 64) + w) * 64 + lane;
    const int row = cidx >> 3, chunk = (cidx & 7) ^ ((row >> 1) & 7);
    __builtin_amdgcn_global_load_lds((const u32*)(g + (size_t)row * ld + chunk * 8), (u32*)(lds + cidx * 16), 16, 0, 0);
  }
}

constexpr int G1_STAGE = (128 + 256) * 128;
constexpr int G1_LDS = 3 * G1_STAGE + 128 * 8;

__global__ __launch_bounds__(256) void gemm1_kernel(const u16* __restrict__ x, const u16* __restrict__ wgu,
                                                    const float* __restrict__ colsum, u16* __restrict__ h) {
  extern __shared__ __attribute__((aligned(16))) char smem[];
  float2* sSt = (float2*)(smem + 3 * G1_STAGE);
  const int t = threadIdx.x, w = t >> 6, lane = t & 63;
  const int bm = blockIdx.x % 16, bn = blockIdx.x / 16;
  const u16* A = x + (size_t)bm * 128 * DM;
  const u16* B = wgu + (size_t)bn * 256 * DM;
  stage_tile<128, 256>(A, DM, smem, t);
  stage_tile<256, 256>(B, DM, smem + 128 * 128, t);
  stage_tile<128, 256>(A + 64, DM, smem + G1_STAGE, t);
  stage_tile<256, 256>(B + 64, DM, smem + G1_STAGE + 128 * 128, t);
  for (int i = 0; i < 32; ++i) {
    const int row = w * 32 + i;
    const uint4 v = *(const uint4*)(A + (size_t)row * DM + lane * 8);
    const u32 vv[4] = {v.x, v.y, v.z, v.w};
    float f[8]; float s = 0.f;
#pragma unroll
    for (int k = 0; k < 4; ++k) { f[2 * k] = bf2f(vv[k] & 0xffff); f[2 * k + 1] = bf2f(vv[k] >> 16); s += f[2 * k] + f[2 * k + 1]; }
    const float mean = wave_sum(s) * (1.0f / DM);
    float q = 0.f;
#pragma unroll
    for (int k = 0; k < 8; ++k) { float dd = f[k] - mean; q += dd * dd; }
    q = wave_sum(q);
    if (lane == 0) { float m_bf, s_bf; ln_scalars(mean, q, m_bf, s_bf); sSt[row] = make_float2(m_bf, 1.0f / s_bf); }
  }
  const int wm = w >> 1, wn = w & 1, r = lane & 31, hh = lane >> 5;
  f32x16 acc[2][4];
#pragma unroll
  for (int i = 0; i < 2; ++i)
#pragma unroll
    for (int j = 0; j < 4; ++j)
#pragma unroll
      for (int e = 0; e < 16; ++e) acc[i][j][e] = 0.f;
  for (int kt = 0; kt < 8; ++kt) {
    if (kt < 7) asm volatile("s_waitcnt vmcnt(12)" ::: "memory");
    else asm volatile("s_waitcnt vmcnt(0)" ::: "memory");
    __syncthreads();
    if (kt + 2 < 8) {
      char* nb = smem + ((kt + 2) % 3) * G1_STAGE;
      stage_tile<128, 256>(A + (kt + 2) * 64, DM, nb, t);
      stage_tile<256, 256>(B + (kt + 2) * 64, DM, nb + 128 * 128, t);
    }
    const char* sa = smem + (kt % 3) * G1_STAGE;
    const char* sb = sa + 128 * 128;
#pragma unroll
    for (int ks = 0; ks < 4; ++ks) {
      bf16x8 af[2], bfr[4];
#pragma unroll
      for (int i = 0; i < 2; ++i) af[i] = *(const bf16x8*)(sa + swz_off(wm * 64 + i * 32 + r, 2 * ks + hh));
#pragma unroll
      for (int j = 0; j < 4; ++j) bfr[j] = *(const bf16x8*)(sb + swz_off(wn * 128 + j * 32 + r, 2 * ks + hh));
#pragma unroll
      for (int i = 0; i < 2; ++i)
#pragma unroll
        for (int j = 0; j < 4; ++j) acc[i][j] = MFMA32(af[i], bfr[j], acc[i][j]);
    }
  }
  const int fb = bn * 2 + wn;
#pragma unroll
  for (int j = 0; j < 2; ++j) {
    const int fcol = fb * 64 + j * 32 + r;
    const float csg = colsum[fb * 128 + j * 32 + r], csu = colsum[fb * 128 + 64 + j * 32 + r];
#pragma unroll
    for (int i = 0; i < 2; ++i)
#pragma unroll
      for (int e = 0; e < 16; ++e) {
        const int row = wm * 64 + i * 32 + (e & 3) + 8 * (e >> 2) + 4 * hh;
        const float2 st = sSt[row];
        const float g = rbf((acc[i][j][e] - st.x * csg) * st.y);
        const float u = rbf((acc[i][j + 2][e] - st.x * csu) * st.y);
        const float inner = 0.7978845608028654f * (g + 0.044715f * g * g * g);
        const float th = 1.0f - 2.0f / (__expf(2.0f * inner) + 1.0f);
        const float ge = rbf(0.5f * g * (1.0f + th));
        h[(size_t)(bm * 128 + row) * FF + fcol] = f2bf(ge * u);
      }
  }
}

constexpr int G2_STAGE = 128 * 128;
constexpr int G2_LDS = 3 * G2_STAGE;

__global__ __launch_bounds__(256) void gemm2_kernel(const u16* __restrict__ hbuf, const u16* __restrict__ wdt,
                                                    const u16* __restrict__ xin, u16* __restrict__ xo,
                                                    float2* __restrict__ stats) {
  extern __shared__ __attribute__((aligned(16))) char smem[];
  const int t = threadIdx.x, w = t >> 6, lane = t & 63;
  const int bn = blockIdx.x % 8, bm = blockIdx.x / 8;
  const u16* A = hbuf + (size_t)bm * 64 * FF;
  const u16* B = wdt + (size_t)bn * 64 * FF;
  constexpr int NK = FF / 64;
  stage_tile<64, 256>(A, FF, smem, t);
  stage_tile<64, 256>(B, FF, smem + 64 * 128, t);
  stage_tile<64, 256>(A + 64, FF, smem + G2_STAGE, t);
  stage_tile<64, 256>(B + 64, FF, smem + G2_STAGE + 64 * 128, t);
  const int wm = w >> 1, wn = w & 1, r = lane & 31, hh = lane >> 5;
  f32x16 acc;
#pragma unroll
  for (int e = 0; e < 16; ++e) acc[e] = 0.f;
  for (int kt = 0; kt < NK; ++kt) {
    if (kt < NK - 1) asm volatile("s_waitcnt vmcnt(4)" ::: "memory");
    else asm volatile("s_waitcnt vmcnt(0)" ::: "memory");
    __syncthreads();
    if (kt + 2 < NK) {
      char* nb = smem + ((kt + 2) % 3) * G2_STAGE;
      stage_tile<64, 256>(A + (kt + 2) * 64, FF, nb, t);
      stage_tile<64, 256>(B + (kt + 2) * 64, FF, nb + 64 * 128, t);
    }
    const char* sa = smem + (kt % 3) * G2_STAGE;
    const char* sb = sa + 64 * 128;
#pragma unroll
    for (int ks = 0; ks < 4; ++ks) {
      const bf16x8 a = *(const bf16x8*)(sa + swz_off(wm * 32 + r, 2 * ks + hh));
      const bf16x8 b = *(const bf16x8*)(sb + swz_off(wn * 32 + r, 2 * ks + hh));
      acc = MFMA32(a, b, acc);
    }
  }
  __syncthreads();
  float* tl = (float*)smem;
#pragma unroll
  for (int e = 0; e < 16; ++e) {
    const int row = wm * 32 + (e & 3) + 8 * (e >> 2) + 4 * hh;
    tl[row * 65 + wn * 32 + r] = acc[e];
  }
  __syncthreads();
  {
    const int row = t >> 2, cq = (t & 3) * 16;
    const size_t g = (size_t)(bm * 64 + row) * DM + bn * 64 + cq;
    const uint4 x0 = *(const uint4*)(xin + g), x1 = *(const uint4*)(xin + g + 8);
    const u32 xv[8] = {x0.x, x0.y, x0.z, x0.w, x1.x, x1.y, x1.z, x1.w};
    float o[16]; u32 ow[8]; float s = 0.f;
#pragma unroll
    for (int k = 0; k < 8; ++k) {
      const float a0 = rbf(bf2f(xv[k] & 0xffff) + rbf(tl[row * 65 + cq + 2 * k]));
      const float a1 = rbf(bf2f(xv[k] >> 16) + rbf(tl[row * 65 + cq + 2 * k + 1]));
      o[2 * k] = a0; o[2 * k + 1] = a1; s += a0 + a1;
      ow[k] = (u32)f2bf(a0) | ((u32)f2bf(a1) << 16);
    }
    *(uint4*)(xo + g) = make_uint4(ow[0], ow[1], ow[2], ow[3]);
    *(uint4*)(xo + g + 8) = make_uint4(ow[4], ow[5], ow[6], ow[7]);
    s += __shfl_xor(s, 1, 64); s += __shfl_xor(s, 2, 64);
    const float mean = s * (1.0f / 64);
    float q = 0.f;
#pragma unroll
    for (int k = 0; k < 16; ++k) { const float dd = o[k] - mean; q += dd * dd; }
    q += __shfl_xor(q, 1, 64); q += __shfl_xor(q, 2, 64);
    if ((t & 3) == 0) stats[(size_t)(bm * 64 + row) * 8 + bn] = make_float2(mean, q);
  }
}

extern "C" void kernel_launch(void* const* d_in, const int* in_sizes, int n_in, void* d_out, int out_size,
                              void* d_ws, size_t ws_size, hipStream_t stream) {
  const u16* x0 = (const u16*)d_in[0];
  char* ws = (char*)d_ws;
  u16* x1 = (u16*)(ws + WS_X1);
  u16* x2 = (u16*)(ws + WS_X2);
  u16* hb = (u16*)(ws + WS_H);
  u16* out = (u16*)d_out;
  hipFuncSetAttribute((const void*)conv_kernel, hipFuncAttributeMaxDynamicSharedMemorySize, CONV_LDS);
  hipFuncSetAttribute((const void*)gemm1_kernel, hipFuncAttributeMaxDynamicSharedMemorySize, G1_LDS);
  hipFuncSetAttribute((const void*)gemm2_kernel, hipFuncAttributeMaxDynamicSharedMemorySize, G2_LDS);
  prep_kernel<<<NB_PREP, PREP_T, 0, stream>>>(x0, (const u16*)d_in[1], (const u16*)d_in[2], (const u16*)d_in[3],
                                              (const u16*)d_in[4], (const u16*)d_in[5], (const u16*)d_in[6],
                                              (const u16*)d_in[7], (const u16*)d_in[10], (const u16*)d_in[11],
                                              (const u16*)d_in[12], ws);
  const u16* dskip = (const u16*)d_in[8];
  const float2* st = (const float2*)(ws + WS_ST);
  conv_kernel<<<DM / 2, 256, CONV_LDS, stream>>>(x0, st, 1, ws + WS_KIMG, dskip, x1);
  gemm1_kernel<<<16 * 14, 256, G1_LDS, stream>>>(x1, (const u16*)(ws + WS_WGU), (const float*)(ws + WS_CS), hb);
  gemm2_kernel<<<32 * 8, 256, G2_LDS, stream>>>(hb, (const u16*)(ws + WS_WD), x1, x2, (float2*)(ws + WS_ST));
  conv_kernel<<<DM / 2, 256, CONV_LDS, stream>>>(x2, st, 8, ws + WS_KIMG, dskip, x1);
  gemm1_kernel<<<16 * 14, 256, G1_LDS, stream>>>(x1, (const u16*)(ws + WS_WGU), (const float*)(ws + WS_CS), hb);
  gemm2_kernel<<<32 * 8, 256, G2_LDS, stream>>>(hb, (const u16*)(ws + WS_WD), x1, out, (float2*)(ws + WS_ST));
}
```

```cpp
#include <hip/hip_runtime.h>
#include <hip/hip_bf16.h>
#include <stdint.h>
#include <type_traits>

typedef unsigned short u16;
typedef unsigned int u32;
using bf16x8 = __attribute__((ext_vector_type(8))) short;
using bf16x4 = __attribute__((ext_vector_type(4))) short;
using f32x16 = __attribute__((ext_vector_type(16))) float;
using u32x4 = __attribute__((ext_vector_type(4))) unsigned;
#define DI __device__ __forceinline__
#define MFMA32(a, b, c) __builtin_amdgcn_mfma_f32_32x32x16_bf16((a), (b), (c), 0, 0, 0)

#define STAMP(kid, i) do {} while (0)
constexpr int SL = 2048;
constexpr int DM = 512;
constexpr int NST = 128;
constexpr int FF = 1792;
constexpr int NGU = 2 * FF;

constexpr size_t KCOPY = 4160;
constexpr size_t KIMG = 4 * KCOPY;
constexpr size_t KSRC = 4176;
constexpr size_t WS_KIMG = 0;
constexpr size_t WS_WGU = WS_KIMG + (size_t)DM * 2 * KSRC;
constexpr size_t WS_WD = WS_WGU + (size_t)NGU * DM * 2;
constexpr size_t WS_CS = WS_WD + (size_t)DM * FF * 2;
constexpr size_t WS_ST = WS_CS + (size_t)NGU * 4;
constexpr size_t WS_X1 = WS_ST + (size_t)SL * 8 * 8;
constexpr size_t WS_X2 = WS_X1 + (size_t)SL * DM * 2;
constexpr size_t WS_H = WS_X2 + (size_t)SL * DM * 2;
constexpr size_t WS_END = WS_H + (size_t)SL * FF * 2;

DI float bf2f(u16 v) { return __uint_as_float((u32)v << 16); }
DI u16 f2bf(float f) {
  u32 u = __float_as_uint(f);
  if ((u & 0x7fffffffu) > 0x7f800000u) return (u16)((u >> 16) | 0x40u);
  u += 0x7fffu + ((u >> 16) & 1u);
  return (u16)(u >> 16);
}
DI float rbf(float f) { return bf2f(f2bf(f)); }
DI u32 pk_bf16(float a, float b) { u32 r; asm("v_cvt_pk_bf16_f32 %0, %1, %2" : "=v"(r) : "v"(a), "v"(b)); return r; }
DI void rbf2(float& a, float& b) { const u32 r = pk_bf16(a, b); a = __uint_as_float(r << 16); b = __uint_as_float(r & 0xffff0000u); }
DI void raw_barrier() { asm volatile("s_waitcnt lgkmcnt(0)" ::: "memory"); __builtin_amdgcn_s_barrier(); asm volatile("" ::: "memory"); }
DI float wave_sum(float v) {
#pragma unroll
  for (int o = 32; o >= 1; o >>= 1) v += __shfl_xor(v, o, 64);
  return v;
}
DI void sincos_small(float x, float& sn, float& cs) {
  const float j = rintf(x * 0.636619772367581343f);
  float r = fmaf(-j, 1.57079637050628662109375f, x);
  r = fmaf(-j, -4.371138828673793e-08f, r);
  r = fmaf(-j, -1.7763568394002505e-15f, r);
  const float z = r * r;
  const float sp = fmaf(r * z, fmaf(z, fmaf(z, -1.9515295891e-4f, 8.3321608736e-3f), -1.6666654611e-1f), r);
  const float cp = fmaf(z * z, fmaf(z, fmaf(z, 2.443315711809948e-5f, -1.388731625493765e-3f), 4.166664568298827e-2f), fmaf(z, -0.5f, 1.0f));
  const int q = (int)j;
  float a = (q & 1) ? cp : sp, b = (q & 1) ? sp : cp;
  sn = __uint_as_float(__float_as_uint(a) ^ ((u32)(q & 2) << 30));
  cs = __uint_as_float(__float_as_uint(b) ^ ((u32)((q + 1) & 2) << 30));
}
struct cf { float r, i; };
DI cf cmul(cf a, cf b) { cf c; c.r = a.r * b.r - a.i * b.i; c.i = a.r * b.i + a.i * b.r; return c; }

DI void ln_scalars(float mean, float m2, float& m_bf, float& s_bf) {
  m_bf = rbf(mean);
  float v_bf = rbf(m2 * (1.0f / DM));
  s_bf = rbf(sqrtf(rbf(v_bf + 1e-5f)));
}

constexpr int PREP_T = 256;
constexpr int NB_KGEN = DM;
constexpr int NB_STAT = SL / 16;
constexpr int NB_TRW = 3 * (DM / 64) * (FF / 64);
constexpr int NB_CS = NGU / 64;
constexpr int NB_PREP = NB_KGEN;

__global__ __launch_bounds__(PREP_T, 2) void prep_kernel(
    const u16* __restrict__ x0, const u16* __restrict__ A_re, const u16* __restrict__ A_im,
    const u16* __restrict__ B_re, const u16* __restrict__ B_im, const u16* __restrict__ C_re,
    const u16* __restrict__ C_im, const u16* __restrict__ log_dt, const u16* __restrict__ wg,
    const u16* __restrict__ wu, const u16* __restrict__ wd, char* __restrict__ ws) {
  constexpr int TP = 272;
  __shared__ __attribute__((aligned(16))) char sTab[2 * (40 + 64) * TP];
  __shared__ float sK[648];
  const int t = threadIdx.x, w = t >> 6, lane = t & 63;
  STAMP(0, 5);
  u16 pAr[2], pAi[2], pBr[2], pBi[2], pCr[2], pCi[2];
  const u16 pLdt = log_dt[blockIdx.x];
#pragma unroll
  for (int h = 0; h < 2; ++h) {
    const int o = blockIdx.x * NST + h * 64 + lane;
    pAr[h] = A_re[o]; pAi[h] = A_im[o]; pBr[h] = B_re[o]; pBi[h] = B_im[o]; pCr[h] = C_re[o]; pCi[h] = C_im[o];
  }
  auto small = [=](int b) __attribute__((always_inline)) {
  if (b < NB_STAT) {
    float2* st = (float2*)(ws + WS_ST);
    uint4 v[4];
#pragma unroll
    for (int i = 0; i < 4; ++i) {
      const int row = b * 16 + w * 4 + i;
      v[i] = *(const uint4*)(x0 + (size_t)row * DM + lane * 8);
    }
#pragma unroll
    for (int i = 0; i < 4; ++i) {
      const int row = b * 16 + w * 4 + i;
      float f[8];
      const u32 vv[4] = {v[i].x, v[i].y, v[i].z, v[i].w};
      float s = 0.f;
#pragma unroll
      for (int k = 0; k < 4; ++k) { f[2 * k] = bf2f(vv[k] & 0xffff); f[2 * k + 1] = bf2f(vv[k] >> 16); s += f[2 * k] + f[2 * k + 1]; }
      const float mean = wave_sum(s) * (1.0f / DM);
      float q = 0.f;
#pragma unroll
      for (int k = 0; k < 8; ++k) { float dd = f[k] - mean; q += dd * dd; }
      q = wave_sum(q);
      if (lane == 0 && row < SL) st[row] = make_float2(mean, q);
      *(uint4*)(sTab + (w * 4 + i) * 1040 + lane * 16) = v[i];
    }
    __syncthreads();
    {
      u32 o0[8], o1[8];
#pragma unroll
      for (int k = 0; k < 8; ++k) {
        const u32 a = *(const u32*)(sTab + (2 * k) * 1040 + t * 4), c = *(const u32*)(sTab + (2 * k + 1) * 1040 + t * 4);
        o0[k] = (a & 0xffff) | (c << 16); o1[k] = (a >> 16) | (c & 0xffff0000u);
      }
      uint4* d0 = (uint4*)((u16*)(ws + WS_X2) + (size_t)(2 * t) * SL + b * 16);
      uint4* d1 = (uint4*)((u16*)(ws + WS_X2) + (size_t)(2 * t + 1) * SL + b * 16);
      d0[0] = make_uint4(o0[0], o0[1], o0[2], o0[3]); d0[1] = make_uint4(o0[4], o0[5], o0[6], o0[7]);
      d1[0] = make_uint4(o1[0], o1[1], o1[2], o1[3]); d1[1] = make_uint4(o1[4], o1[5], o1[6], o1[7]);
    }
    return;
  }
  b -= NB_STAT;
  if (b < NB_TRW) {
    u16(*tl)[66] = (u16(*)[66])sTab;
    const int which = b / 224, tb = b % 224;
    const u16* src; u16* dst; int ld_src, ld_dst, r0, c0, dst_row0;
    if (which < 2) {
      src = which == 0 ? wg : wu; asm volatile("" : "+s"(src)); ld_src = FF;
      r0 = (tb / 28) * 64; c0 = (tb % 28) * 64;
      dst = (u16*)(ws + WS_WGU); ld_dst = DM;
      dst_row0 = (c0 / 32) * 64 + which * 32;
    } else {
      src = wd; ld_src = DM;
      r0 = (tb / 8) * 64; c0 = (tb % 8) * 64;
      dst = (u16*)(ws + WS_WD); ld_dst = FF;
      dst_row0 = c0;
    }
    if (t < 256) {
      const int row = t >> 2, cq = (t & 3) * 16;
      const uint4 v0 = *(const uint4*)(src + (size_t)(r0 + row) * ld_src + c0 + cq);
      const uint4 v1 = *(const uint4*)(src + (size_t)(r0 + row) * ld_src + c0 + cq + 8);
      const u32 vv[8] = {v0.x, v0.y, v0.z, v0.w, v1.x, v1.y, v1.z, v1.w};
#pragma unroll
      for (int k = 0; k < 8; ++k) { tl[row][cq + 2 * k] = vv[k] & 0xffff; tl[row][cq + 2 * k + 1] = vv[k] >> 16; }
    }
    __syncthreads();
    if (t < 256) {
      const int oc = t >> 2, rq = (t & 3) * 16;
      u32 o[8];
#pragma unroll
      for (int k = 0; k < 8; ++k) o[k] = (u32)tl[rq + 2 * k][oc] | ((u32)tl[rq + 2 * k + 1][oc] << 16);
      const int drow = which < 2 ? dst_row0 + (oc >> 5) * 64 + (oc & 31) : dst_row0 + oc;
      if (which < 2) {
        uint4* dp = (uint4*)(dst + (size_t)drow * ld_dst + r0 + rq);
        dp[0] = make_uint4(o[0], o[1], o[2], o[3]);
        dp[1] = make_uint4(o[4], o[5], o[6], o[7]);
      } else {
        uint4* dp = (uint4*)dst + ((size_t)(drow >> 5) * (FF / 16) + ((r0 + rq) >> 4)) * 64 + (drow & 31);
        dp[0] = make_uint4(o[0], o[1], o[2], o[3]);
        dp[32] = make_uint4(o[4], o[5], o[6], o[7]);
      }
    }
    return;
  }
  b -= NB_TRW;
  {
    float* red = (float*)sTab;
    const int which = b / 28, f0 = (b % 28) * 64;
    const u16* src = which == 0 ? wg : wu; asm volatile("" : "+s"(src));
    if (t < 256) {
      const int c8 = t & 7, rg = t >> 3;
      uint4 v[16];
#pragma unroll
      for (int i = 0; i < 16; ++i) v[i] = *(const uint4*)(src + (size_t)(rg + 32 * i) * FF + f0 + c8 * 8);
      float s[8];
#pragma unroll
      for (int k = 0; k < 8; ++k) s[k] = 0.f;
#pragma unroll
      for (int i = 0; i < 16; ++i) {
        const u32 vv[4] = {v[i].x, v[i].y, v[i].z, v[i].w};
#pragma unroll
        for (int k = 0; k < 4; ++k) { s[2 * k] += bf2f(vv[k] & 0xffff); s[2 * k + 1] += bf2f(vv[k] >> 16); }
      }
#pragma unroll
      for (int k = 0; k < 8; ++k) red[rg * 64 + c8 * 8 + k] = s[k];
    }
    __syncthreads();
    if (t < 64) {
      float a = 0.f;
#pragma unroll
      for (int g = 0; g < 32; ++g) a += red[g * 64 + t];
      float* cs = (float*)(ws + WS_CS);
      cs[((f0 + t) >> 5) * 64 + which * 32 + (t & 31)] = a;
    }
  }
  };
  for (int tb = blockIdx.x; tb < NB_STAT + NB_TRW + NB_CS; tb += NB_KGEN) { small(tb); __syncthreads(); }
  {
    STAMP(0, 0);
    const int d = blockIdx.x;
    const float dt = rbf(expf(bf2f(pLdt)));
    char* QA = (char*)sTab;
    char* RB = QA + 2 * 40 * TP;
    using f32x4 = __attribute__((ext_vector_type(4))) float;
    f32x4 acc = {0.f, 0.f, 0.f, 0.f};
    const int nl = t & 63;
    float lsum = 0.f;
#pragma unroll 1
    for (int half = 0; half < 2; ++half) {
      const float lr = dt * bf2f(half ? pAr[1] : pAr[0]);
      const float li = dt * bf2f(half ? pAi[1] : pAi[0]);
      auto put = [=](char* base, int part_rows, int row, float re, float im) __attribute__((always_inline)) {
        const u32 hi = pk_bf16(re, im);
        const float rl = re - __uint_as_float(hi << 16), il = im - __uint_as_float(hi & 0xffff0000u);
        *(u32*)(base + row * TP + nl * 4) = hi;
        *(u32*)(base + (part_rows + row) * TP + nl * 4) = pk_bf16(rl, il);
      };
      auto tables = [=, &lsum](auto fast_tag) __attribute__((always_inline)) {
      constexpr bool FAST = decltype(fast_tag)::value;
      auto cexp2 = [=](int k) __attribute__((always_inline)) {
        const float sc = (float)(1 << k);
        float er = expf(lr * sc), sn, cs;
        if constexpr (FAST) sincos_small(li * sc, sn, cs); else sincosf(li * sc, &sn, &cs);
        cf e; e.r = er * cs; e.i = er * sn;
        return e;
      };
      if (w < 2) {
        cf E[5];
#pragma unroll
        for (int k = 0; k < 5; ++k) E[k] = cexp2(2 * w + k);
#pragma unroll
        for (int tq = 0; tq < 2; ++tq) {
          const int tau = 2 * w + tq;
          cf rr[16];
          rr[0].r = 1.f; rr[0].i = 0.f;
          rr[1] = E[tq]; rr[2] = E[tq + 1]; rr[3] = cmul(rr[2], E[tq]);
          rr[4] = E[tq + 2]; rr[5] = cmul(rr[4], E[tq]); rr[6] = cmul(rr[4], E[tq + 1]);
          rr[7] = cmul(rr[6], E[tq]); rr[8] = E[tq + 3];
#pragma unroll
          for (int q = 1; q < 8; ++q) rr[8 + q] = cmul(rr[8], rr[q]);
#pragma unroll
          for (int q = 0; q < 16; ++q) put(RB, 64, tau * 16 + q, rr[q].r, rr[q].i);
        }
      } else {
        const float dBr = dt * bf2f(half ? pBr[1] : pBr[0]), dBi = dt * bf2f(half ? pBi[1] : pBi[0]);
        const float Cr = bf2f(half ? pCr[1] : pCr[0]), Ci = bf2f(half ? pCi[1] : pCi[0]);
        cf W; W.r = Cr * dBr - Ci * dBi; W.i = Cr * dBi + Ci * dBr;
        auto qrows = [=](cf base, cf e0, cf e1, cf e2, int row0) __attribute__((always_inline)) {
          cf qv[8];
          qv[0] = base; qv[1] = cmul(base, e0); qv[2] = cmul(base, e1); qv[3] = cmul(qv[2], e0);
          qv[4] = cmul(base, e2); qv[5] = cmul(qv[4], e0); qv[6] = cmul(qv[4], e1); qv[7] = cmul(qv[6], e0);
#pragma unroll
          for (int p = 0; p < 8; ++p) put(QA, 40, row0 + p, qv[p].r, -qv[p].i);
        };
        if (w == 2) {
          const cf e4 = cexp2(4), e5 = cexp2(5), e6 = cexp2(6), e7 = cexp2(7);
          qrows(W, e4, e5, e6, 0);
          qrows(cmul(W, e7), e4, e5, e6, 8);
          const cf e11 = cexp2(11);
          lsum += W.r * e11.r - W.i * e11.i;
        } else {
          cf E[6];
#pragma unroll
          for (int k = 0; k < 6; ++k) E[k] = cexp2(5 + k);
#pragma unroll
          for (int tau = 1; tau < 4; ++tau)
            qrows(cmul(W, E[tau + 2]), E[tau - 1], E[tau], E[tau + 1], 8 + tau * 8);
        }
      }
      };
      if (half) { __syncthreads(); STAMP(0, 7); }
      if (__all(fabsf(li) * 2048.f < 8192.f)) tables(std::integral_constant<bool, true>{});
      else tables(std::integral_constant<bool, false>{});
      if (half == 0) STAMP(0, 2);
      __syncthreads();
      if (half == 0) STAMP(0, 6);
      {
        const int tau = w, m = lane & 15, kq = lane >> 4;
        const int qrow = tau == 0 ? m : 8 + tau * 8 + (m & 7);
#pragma unroll
        for (int k2 = 0; k2 < 4; ++k2) {
          const int off = k2 * 64 + kq * 16;
          const bf16x8 ah = *(const bf16x8*)(QA + qrow * TP + off);
          const bf16x8 al = *(const bf16x8*)(QA + (40 + qrow) * TP + off);
          const bf16x8 bh = *(const bf16x8*)(RB + (tau * 16 + m) * TP + off);
          const bf16x8 bl = *(const bf16x8*)(RB + (64 + tau * 16 + m) * TP + off);
          acc = __builtin_amdgcn_mfma_f32_16x16x32_bf16(al, bh, acc, 0, 0, 0);
          acc = __builtin_amdgcn_mfma_f32_16x16x32_bf16(ah, bl, acc, 0, 0, 0);
          acc = __builtin_amdgcn_mfma_f32_16x16x32_bf16(ah, bh, acc, 0, 0, 0);
        }
      }
    }
    STAMP(0, 1);
    {
      const int tau = w, m = lane & 15, kq = lane >> 4;
      if (w == 2) { lsum = wave_sum(lsum); if (lane == 0) sK[640] = lsum; }
#pragma unroll
      for (int i = 0; i < 4; ++i) {
        const int p = kq * 4 + i;
        if (tau == 0) sK[(p >> 3) * 128 + (p & 7) * 16 + m] = acc[i];
        else if (p < 8) sK[(tau + 1) * 128 + p * 16 + m] = acc[i];
      }
    }
    __syncthreads();
    STAMP(0, 3);
    u32* img = (u32*)(ws + WS_KIMG + (size_t)d * 2 * KSRC);
#pragma unroll
    for (int it = 0; it < 5; ++it) {
      const int j = t + it * PREP_T;
      if (j < 1044) {
        u32 oh = 0, ol = 0;
#pragma unroll
        for (int h = 0; h < 2; ++h) {
          const int l = 2047 - (2 * j + h);
          float kv = 0.f;
          if (l >= 0) {
            const int v = (int)rbf((float)l);
            int idx;
            if (v < 256) idx = v;
            else if (v < 512) idx = 256 + ((v - 256) >> 1);
            else if (v < 1024) idx = 384 + ((v - 512) >> 2);
            else if (v < 2048) idx = 512 + ((v - 1024) >> 3);
            else idx = 640;
            kv = sK[idx];
          }
          const u16 hi = f2bf(kv);
          oh |= (u32)hi << (16 * h);
          ol |= (u32)f2bf(kv - bf2f(hi)) << (16 * h);
        }
        img[j] = oh;
        img[1044 + j] = ol;
      }
    }
    STAMP(0, 4);
  }
}

constexpr int UIMG = 96 * 64;
constexpr int CONV_T = 512;
constexpr int CONV_LDS = 4 * KIMG + 2 * UIMG + SL * 8 + 2 * 2 * 32 * 65 * 4;

template <bool TIN>
__global__ __launch_bounds__(CONV_T) void conv_kernel(const u16* __restrict__ x, const float2* __restrict__ stats,
                                                      int P, const char* __restrict__ ksrc,
                                                      const u16* __restrict__ dskip, u16* __restrict__ xout) {
  extern __shared__ __attribute__((aligned(16))) char smem[];
  char* sKI = smem;
  char* sU = smem + 4 * KIMG;
  float2* sSt = (float2*)(smem + 4 * KIMG + 2 * UIMG);
  float* sY = (float*)(smem + 4 * KIMG + 2 * UIMG + SL * 8);
  const int t = threadIdx.x, w = t >> 6, lane = t & 63;
  const int ch0 = blockIdx.x * 2;
  const int img = w >> 1, par = w & 1;
  STAMP(1, 0);
  u32 xv[4];
#pragma unroll
  for (int i = 0; i < 4; ++i) xv[i] = 0;
  if (TIN) {
    const uint2 a = *(const uint2*)(x + (size_t)ch0 * SL + 4 * t), b = *(const uint2*)(x + (size_t)(ch0 + 1) * SL + 4 * t);
    xv[0] = (a.x & 0xffff) | (b.x << 16); xv[1] = (a.x >> 16) | (b.x & 0xffff0000u);
    xv[2] = (a.y & 0xffff) | (b.y << 16); xv[3] = (a.y >> 16) | (b.y & 0xffff0000u);
  } else {
#pragma unroll
    for (int i = 0; i < 4; ++i) xv[i] = *(const u32*)(x + (size_t)(4 * t + i) * DM + ch0);
  }
  u32 ks[18];
  {
    const u32* src = (const u32*)(ksrc + (size_t)(ch0 * 2 + img) * KSRC) + lane * 16;
#pragma unroll
    for (int q = 0; q < 4; ++q) {
      const uint4 v = *(const uint4*)(src + 4 * q);
      ks[4 * q] = v.x; ks[4 * q + 1] = v.y; ks[4 * q + 2] = v.z; ks[4 * q + 3] = v.w;
    }
    ks[16] = src[16]; ks[17] = src[17];
  }
  u32 kx0 = 0, kx1 = 0, kx2 = 0;
  if (lane < 16) {
    const u32* src = (const u32*)(ksrc + (size_t)(ch0 * 2 + img) * KSRC) + 1024 + lane;
    kx0 = src[0]; kx1 = src[1]; kx2 = src[2];
  }
  if (P == 1) {
    float2 sv[4];
#pragma unroll
    for (int i = 0; i < 4; ++i) sv[i] = stats[t + 512 * i];
#pragma unroll
    for (int i = 0; i < 4; ++i) {
      float m_bf, s_bf; ln_scalars(sv[i].x, sv[i].y, m_bf, s_bf);
      sSt[t + 512 * i] = make_float2(m_bf, s_bf);
    }
  } else {
    float4 sv[4][4];
#pragma unroll
    for (int i = 0; i < 4; ++i)
#pragma unroll
      for (int p = 0; p < 4; ++p) sv[i][p] = *(const float4*)(stats + (size_t)(t + 512 * i) * 8 + 2 * p);
#pragma unroll
    for (int i = 0; i < 4; ++i) {
      float mean = 0.f, q = 0.f;
#pragma unroll
      for (int p = 0; p < 4; ++p) { mean += sv[i][p].x + sv[i][p].z; q += sv[i][p].y + sv[i][p].w; }
      mean *= 0.125f;
#pragma unroll
      for (int p = 0; p < 4; ++p) {
        float d0 = sv[i][p].x - mean, d1 = sv[i][p].z - mean;
        q += 64.f * (d0 * d0 + d1 * d1);
      }
      float m_bf, s_bf; ln_scalars(mean, q, m_bf, s_bf);
      sSt[t + 512 * i] = make_float2(m_bf, s_bf);
    }
  }
  STAMP(1, 6);
  if (t < 2 * 32 * 4) *(uint4*)(sU + (t >> 7) * UIMG + (t & 127) * 16) = make_uint4(0, 0, 0, 0);
  {
    char* dst = sKI + img * KIMG;
#pragma unroll
    for (int cc = 0; cc < 2; ++cc) {
      const int c = par + 2 * cc;
      u32 o[16];
#pragma unroll
      for (int j = 0; j < 16; ++j)
        o[j] = par == 0 ? ks[j + cc] : __builtin_amdgcn_alignbit(ks[j + cc + 1], ks[j + cc], 16);
      char* dp = dst + c * KCOPY + lane * 64;
#pragma unroll
      for (int q = 0; q < 4; ++q) *(uint4*)(dp + 16 * q) = make_uint4(o[4 * q], o[4 * q + 1], o[4 * q + 2], o[4 * q + 3]);
      if (lane < 16) {
        const u32 e0 = cc == 0 ? kx0 : kx1, e1 = cc == 0 ? kx1 : kx2;
        *(u32*)(dst + c * KCOPY + 4096 + lane * 4) = par == 0 ? e0 : __builtin_amdgcn_alignbit(e1, e0, 16);
      }
    }
  }
  STAMP(1, 7);
  __syncthreads();
  STAMP(1, 1);
  u32 uv[4];
  {
    const float4 sa = *(const float4*)(sSt + 4 * t), sb = *(const float4*)(sSt + 4 * t + 2);
    const float mm[4] = {sa.x, sa.z, sb.x, sb.z}, ss[4] = {sa.y, sa.w, sb.y, sb.w};
#pragma unroll
    for (int i = 0; i < 4; ++i) {
      u32 pk = 0;
#pragma unroll
      for (int c = 0; c < 2; ++c) {
        const float xf = bf2f(c ? (xv[i] >> 16) : (xv[i] & 0xffff));
        pk |= (u32)f2bf(rbf(xf - mm[i]) / ss[i]) << (16 * c);
      }
      uv[i] = pk;
    }
    const int row = (t >> 3) + 32, ch8 = (t & 7) >> 1;
    const int off = row * 64 + ((ch8 ^ ((row >> 2) & 3)) << 4) + (t & 1) * 8;
    *(uint2*)(sU + off) = make_uint2((uv[0] & 0xffff) | (uv[1] << 16), (uv[2] & 0xffff) | (uv[3] << 16));
    *(uint2*)(sU + UIMG + off) = make_uint2((uv[0] >> 16) | (uv[1] & 0xffff0000u), (uv[2] >> 16) | (uv[3] & 0xffff0000u));
  }
  __syncthreads();
  STAMP(1, 2);
  const int m = lane & 31, kg = lane >> 5;
  const int chl = __builtin_amdgcn_readfirstlane(w >> 2), dq = __builtin_amdgcn_readfirstlane(w & 3);
  const int s0 = 2047 + 8 * kg - m, c0 = s0 & 3;
  const char* abase = sKI + chl * 2 * KIMG + c0 * KCOPY + (s0 - c0) * 2;
  const char* ubase = sU + chl * UIMG;
  f32x16 acc0, acc1;
#pragma unroll
  for (int i = 0; i < 16; ++i) { acc0[i] = 0.f; acc1[i] = 0.f; }
  auto afrag = [=](int part, int dl, int kq) __attribute__((always_inline)) -> bf16x8 {
    const char* p = abase + part * KIMG - 64 * dl + 32 * kq;
    const bf16x4 lo = *(const bf16x4*)p, hi = *(const bf16x4*)(p + 8);
    bf16x8 r; r[0] = lo[0]; r[1] = lo[1]; r[2] = lo[2]; r[3] = lo[3]; r[4] = hi[0]; r[5] = hi[1]; r[6] = hi[2]; r[7] = hi[3];
    return r;
  };
  auto ufrag = [=](int w0, int kq) __attribute__((always_inline)) -> bf16x8 {
    const int row = w0 + m + 32;
    return *(const bf16x8*)(ubase + row * 64 + (((2 * kq + kg) ^ ((row >> 2) & 3)) << 4));
  };
  bf16x8 fr[2][6];
  auto ldstep = [=](int st, bf16x8* f) __attribute__((always_inline)) {
    const int dl = dq + 4 * (st >> 1), kq = st & 1;
    f[0] = ufrag(-dl, kq); f[1] = ufrag(32 - dl, kq);
    f[2] = afrag(1, dl, kq); f[3] = afrag(1, dl + 32, kq);
    f[4] = afrag(0, dl, kq); f[5] = afrag(0, dl + 32, kq);
  };
  ldstep(0, fr[0]);
#pragma unroll
  for (int st = 0; st < 16; ++st) {
    if (st + 1 < 16) ldstep(st + 1, fr[(st + 1) & 1]);
    __builtin_amdgcn_sched_barrier(0);
    const bf16x8* f = fr[st & 1];
    acc0 = MFMA32(f[2], f[0], acc0);
    acc1 = MFMA32(f[2], f[1], acc1);
    acc1 = MFMA32(f[3], f[0], acc1);
    acc0 = MFMA32(f[4], f[0], acc0);
    acc1 = MFMA32(f[4], f[1], acc1);
    acc1 = MFMA32(f[5], f[0], acc1);
    __builtin_amdgcn_sched_barrier(0);
  }
  STAMP(1, 3);
  {
    float* yb = sY + ((dq & 1) * 2 + chl) * 32 * 65;
    if (dq >= 2) {
#pragma unroll
      for (int i = 0; i < 16; ++i) {
        const int a = (i & 3) + 8 * (i >> 2) + 4 * kg;
        yb[a * 65 + m] = acc0[i];
        yb[a * 65 + 32 + m] = acc1[i];
      }
    }
    __syncthreads();
    if (dq < 2) {
#pragma unroll
      for (int i = 0; i < 16; ++i) {
        const int a = (i & 3) + 8 * (i >> 2) + 4 * kg;
        yb[a * 65 + m] += acc0[i];
        yb[a * 65 + 32 + m] += acc1[i];
      }
    }
    __syncthreads();
  }
  STAMP(1, 4);
  const float dk0 = bf2f(dskip[ch0]), dk1 = bf2f(dskip[ch0 + 1]);
  u32 ov[4];
#pragma unroll
  for (int i = 0; i < 4; ++i) {
    const int a = 4 * (t & 7) + i, c = t >> 3;
    u32 out = 0;
#pragma unroll
    for (int k = 0; k < 2; ++k) {
      const float y = rbf(sY[k * 32 * 65 + a * 65 + c] + sY[(2 + k) * 32 * 65 + a * 65 + c]);
      const float u = bf2f(k ? (uv[i] >> 16) : (uv[i] & 0xffff));
      const float sk = rbf(u * (k ? dk1 : dk0));
      const float y2 = rbf(y + sk);
      const float xf = bf2f(k ? (xv[i] >> 16) : (xv[i] & 0xffff));
      out |= (u32)f2bf(xf + y2) << (16 * k);
    }
    ov[i] = out;
  }
  *(uint2*)(xout + (size_t)ch0 * SL + 4 * t) = make_uint2((ov[0] & 0xffff) | (ov[1] << 16), (ov[2] & 0xffff) | (ov[3] << 16));
  *(uint2*)(xout + (size_t)(ch0 + 1) * SL + 4 * t) = make_uint2((ov[0] >> 16) | (ov[1] & 0xffff0000u), (ov[2] >> 16) | (ov[3] & 0xffff0000u));
  STAMP(1, 5);
}

DI int swz_off(int row, int chunk) { return row * 128 + ((chunk ^ ((row >> 1) & 7)) << 4); }

template <int ROWS, int NT>
DI void stage_tile(const u16* g, int ld, char* lds, int t) {
  constexpr int N = ROWS * 8 / NT;
  const int w = t >> 6, lane = t & 63;
#pragma unroll
  for (int i = 0; i < N; ++i) {
    const int cidx = (i * (NT / 64) + w) * 64 + lane;
    const int row = cidx >> 3, chunk = (cidx & 7) ^ ((row >> 1) & 7);
    __builtin_amdgcn_global_load_lds((const u32*)(g + (size_t)row * ld + chunk * 8), (u32*)(lds + cidx * 16), 16, 0, 0);
  }
}

constexpr int G1_STAGE = (128 + 256) * 128;
constexpr int G1_LDS = 3 * G1_STAGE + 128 * 8;

__global__ __launch_bounds__(512) void gemm1_kernel(const u16* __restrict__ x, const u16* __restrict__ wgu,
                                                    const float* __restrict__ colsum, u16* __restrict__ h) {
  extern __shared__ __attribute__((aligned(16))) char smem[];
  float2* sSt = (float2*)(smem + 3 * G1_STAGE);
  const int t = threadIdx.x, w = t >> 6, lane = t & 63;
  const int xcd = blockIdx.x & 7, bidx = blockIdx.x >> 3;
  const int bm = (xcd & 3) * 4 + (bidx & 3), bn = (xcd >> 2) * 7 + (bidx >> 2);
  const u16* A = x + bm * 128;
  auto stageA = [&](int kt, char* dst) __attribute__((always_inline)) {
#pragma unroll
    for (int i = 0; i < 2; ++i) {
      const int cidx = (i * 8 + w) * 64 + lane, k = cidx >> 4, ch = (cidx & 15) ^ ((k & 3) << 2);
      __builtin_amdgcn_global_load_lds((const u32*)(A + (size_t)(kt * 64 + k) * SL + ch * 8), (u32*)(dst + cidx * 16), 16, 0, 0);
    }
  };
  const u16* B = wgu + (size_t)bn * 256 * DM;
  STAMP(2, 0);
  stageA(0, smem);
  stage_tile<256, 512>(B, DM, smem + 128 * 128, t);
  stageA(1, smem + G1_STAGE);
  stage_tile<256, 512>(B + 64, DM, smem + G1_STAGE + 128 * 128, t);
  const int wm = w >> 2, wn = w & 3, r = lane & 31, hh = lane >> 5;
  const float csg = colsum[(bn * 4 + wn) * 64 + r], csu = colsum[(bn * 4 + wn) * 64 + 32 + r];
  f32x16 acc[2][2];
#pragma unroll
  for (int i = 0; i < 2; ++i)
#pragma unroll
    for (int j = 0; j < 2; ++j)
#pragma unroll
      for (int e = 0; e < 16; ++e) acc[i][j][e] = 0.f;
  float shv[8], s1[8], s2[8];
#pragma unroll
  for (int e = 0; e < 8; ++e) { shv[e] = 0.f; s1[e] = 0.f; s2[e] = 0.f; }
  const int g = lane >> 4;
  struct Frag { bf16x4 pr[4]; bf16x8 b[2]; };
  auto ldfrag = [=](unsigned sa, int kq, Frag& f) __attribute__((always_inline)) {
    unsigned ad[4], bd[2];
#pragma unroll
    for (int z = 0; z < 4; ++z) {
      const int col = wm * 64 + (z >> 1) * 32 + 16 * (g & 1) + 4 * (lane & 3);
      const int k = kq * 16 + 8 * (g >> 1) + 4 * (z & 1) + ((lane & 15) >> 2);
      ad[z] = sa + k * 256 + (((col >> 3) ^ ((k & 3) << 2)) << 4) + (col & 7) * 2;
    }
#pragma unroll
    for (int j = 0; j < 2; ++j) bd[j] = sa + 128 * 128 + swz_off(wn * 64 + j * 32 + r, 2 * kq + hh);
    asm volatile("ds_read_b64_tr_b16 %0, %6\n\tds_read_b64_tr_b16 %1, %7\n\tds_read_b64_tr_b16 %2, %8\n\tds_read_b64_tr_b16 %3, %9\n\t"
                 "ds_read_b128 %4, %10\n\tds_read_b128 %5, %11"
                 : "=&v"(f.pr[0]), "=&v"(f.pr[1]), "=&v"(f.pr[2]), "=&v"(f.pr[3]), "=&v"(f.b[0]), "=&v"(f.b[1])
                 : "v"(ad[0]), "v"(ad[1]), "v"(ad[2]), "v"(ad[3]), "v"(bd[0]), "v"(bd[1]));
  };
  for (int kt = 0; kt < 8; ++kt) {
    if (kt < 7) asm volatile("s_waitcnt vmcnt(6)" ::: "memory");
    else asm volatile("s_waitcnt vmcnt(0)" ::: "memory");
    raw_barrier();
    if (kt == 0) STAMP(2, 1);
    if (kt + 2 < 8) {
      char* nb = smem + ((kt + 2) % 3) * G1_STAGE;
      stageA(kt + 2, nb);
      stage_tile<256, 512>(B + (kt + 2) * 64, DM, nb + 128 * 128, t);
    }
    const unsigned sa = (kt % 3) * G1_STAGE;
    Frag fr[2];
    u32x4 sv[2], sv0;
    {
      const int k0 = t >> 4, k1 = k0 + 32;
      const unsigned a0 = sa + k0 * 256 + (((t & 15) ^ ((k0 & 3) << 2)) << 4), a1 = sa + k1 * 256 + (((t & 15) ^ ((k1 & 3) << 2)) << 4);
      const unsigned a2 = sa + ((t & 15) << 4);
      asm volatile("ds_read_b128 %0, %3\n\tds_read_b128 %1, %4\n\tds_read_b128 %2, %5"
                   : "=&v"(sv0), "=&v"(sv[0]), "=&v"(sv[1]) : "v"(a2), "v"(a0), "v"(a1));
    }
    ldfrag(sa, 0, fr[0]);
#pragma unroll
    for (int kq = 0; kq < 4; ++kq) {
      Frag& f = fr[kq & 1];
      if (kq + 1 < 4) {
        ldfrag(sa, kq + 1, fr[(kq + 1) & 1]);
        asm volatile("s_waitcnt lgkmcnt(6)" : "+v"(f.pr[0]), "+v"(f.pr[1]), "+v"(f.pr[2]), "+v"(f.pr[3]), "+v"(f.b[0]), "+v"(f.b[1]),
                     "+v"(sv0), "+v"(sv[0]), "+v"(sv[1]));
      } else {
        asm volatile("s_waitcnt lgkmcnt(0)" : "+v"(f.pr[0]), "+v"(f.pr[1]), "+v"(f.pr[2]), "+v"(f.pr[3]), "+v"(f.b[0]), "+v"(f.b[1]));
      }
      if (kt == 0 && kq == 0) {
        const u32 vv[4] = {sv0[0], sv0[1], sv0[2], sv0[3]};
#pragma unroll
        for (int e = 0; e < 4; ++e) { shv[2 * e] = __uint_as_float(vv[e] << 16); shv[2 * e + 1] = __uint_as_float(vv[e] & 0xffff0000u); }
      }
      bf16x8 af[2];
#pragma unroll
      for (int z = 0; z < 2; ++z)
#pragma unroll
        for (int e = 0; e < 4; ++e) { af[z][e] = f.pr[2 * z][e]; af[z][4 + e] = f.pr[2 * z + 1][e]; }
#pragma unroll
      for (int i = 0; i < 2; ++i)
#pragma unroll
        for (int j = 0; j < 2; ++j) acc[i][j] = MFMA32(af[i], f.b[j], acc[i][j]);
      {
        using f32x2 = __attribute__((ext_vector_type(2))) float;
#pragma unroll
        for (int e2 = 0; e2 < 2; ++e2) {
          const int e = (kq & 1) * 2 + e2;
          const u32 vv = sv[kq >> 1][e];
          f32x2 d = {__uint_as_float(vv << 16), __uint_as_float(vv & 0xffff0000u)};
          f32x2 sh = {shv[2 * e], shv[2 * e + 1]}, a1 = {s1[2 * e], s1[2 * e + 1]}, a2 = {s2[2 * e], s2[2 * e + 1]};
          d -= sh; a1 += d; a2 = __builtin_elementwise_fma(d, d, a2);
          s1[2 * e] = a1[0]; s1[2 * e + 1] = a1[1]; s2[2 * e] = a2[0]; s2[2 * e + 1] = a2[1];
        }
      }
    }
  }
  STAMP(2, 2);
  {
    float2* sp = (float2*)smem;
    raw_barrier();
#pragma unroll
    for (int e = 0; e < 8; e += 2)
      *(float4*)(sp + (t >> 4) * 128 + (t & 15) * 8 + e) = make_float4(s1[e], s2[e], s1[e + 1], s2[e + 1]);
    if (t < 16) {
#pragma unroll
      for (int e = 0; e < 8; ++e) ((float*)(sp + 32 * 128))[t * 8 + e] = shv[e];
    }
    __syncthreads();
    if (t < 128) {
      float a1 = 0.f, a2 = 0.f;
#pragma unroll
      for (int q = 0; q < 32; ++q) { const float2 v = sp[q * 128 + t]; a1 += v.x; a2 += v.y; }
      const float mean = ((const float*)(sp + 32 * 128))[t] + a1 * (1.0f / DM);
      const float M2 = a2 - a1 * a1 * (1.0f / DM);
      float m_bf, s_bf; ln_scalars(mean, M2, m_bf, s_bf); sSt[t] = make_float2(m_bf, 1.0f / s_bf);
    }
  }
  __syncthreads();
  char* ot = smem;
  {
    float4 stv[2][8];
#pragma unroll
    for (int i = 0; i < 2; ++i)
#pragma unroll
      for (int e = 0; e < 16; e += 2) stv[i][e >> 1] = *(const float4*)(sSt + wm * 64 + i * 32 + (e & 3) + 8 * (e >> 2) + 4 * hh);
    __builtin_amdgcn_sched_barrier(0);
#pragma unroll
    for (int i = 0; i < 2; ++i)
#pragma unroll
      for (int e = 0; e < 16; e += 2) {
        const int row = wm * 64 + i * 32 + (e & 3) + 8 * (e >> 2) + 4 * hh;
        const float2 st0 = make_float2(stv[i][e >> 1].x, stv[i][e >> 1].y), st1 = make_float2(stv[i][e >> 1].z, stv[i][e >> 1].w);
        float g0 = (acc[i][0][e] - st0.x * csg) * st0.y, g1 = (acc[i][0][e + 1] - st1.x * csg) * st1.y;
        float u0 = (acc[i][1][e] - st0.x * csu) * st0.y, u1 = (acc[i][1][e + 1] - st1.x * csu) * st1.y;
        rbf2(g0, g1); rbf2(u0, u1);
        const float c1 = 0.7978845608028654f * 2.885390081777927f, c2 = c1 * 0.044715f;
        const float e0 = __builtin_amdgcn_exp2f(g0 * fmaf(c2, g0 * g0, c1));
        const float e1 = __builtin_amdgcn_exp2f(g1 * fmaf(c2, g1 * g1, c1));
        float a0 = g0 - g0 * __builtin_amdgcn_rcpf(e0 + 1.0f), a1 = g1 - g1 * __builtin_amdgcn_rcpf(e1 + 1.0f);
        rbf2(a0, a1);
        const u32 pk = pk_bf16(a0 * u0, a1 * u1);
        *(u16*)(ot + row * 264 + (wn * 32 + r) * 2) = (u16)(pk & 0xffff);
        *(u16*)(ot + (row + 1) * 264 + (wn * 32 + r) * 2) = (u16)(pk >> 16);
      }
  }
  __syncthreads();
#pragma unroll
  for (int i = 0; i < 4; ++i) {
    const int row = (w >> 1) * 32 + (lane & 31), c16 = (w & 1) * 8 + i * 2 + (lane >> 5);
    const uint2 lo = *(const uint2*)(ot + row * 264 + c16 * 16), hi = *(const uint2*)(ot + row * 264 + c16 * 16 + 8);
    ((uint4*)h)[((size_t)(bm * 4 + (w >> 1)) * (FF / 16) + bn * 8 + (c16 >> 1)) * 64 + lane] = make_uint4(lo.x, lo.y, hi.x, hi.y);
  }
  STAMP(2, 3);
}

constexpr int G2_LDS = 8 * 64 * 68 * 4;

template <bool FINAL>
__global__ __launch_bounds__(512) void gemm2_kernel(const u16* __restrict__ hbuf, const u16* __restrict__ wdt,
                                                    const u16* __restrict__ xin, u16* __restrict__ xo,
                                                    float2* __restrict__ stats) {
  extern __shared__ __attribute__((aligned(16))) char smem[];
  const int t = threadIdx.x, w = t >> 6, lane = t & 63;
  const int bm = (blockIdx.x & 7) * 4 + ((blockIdx.x >> 3) & 3), bn = blockIdx.x >> 5;
  constexpr int NS = FF / 16;
  STAMP(3, 0);
  const bf16x8* A = (const bf16x8*)hbuf + ((size_t)(bm * 2) * NS + w) * 64 + lane;
  const bf16x8* B = (const bf16x8*)wdt + ((size_t)(bn * 2) * NS + w) * 64 + lane;
  const int r = lane & 31, hh = lane >> 5;
  constexpr int NIT = NS / 8, PD = 4;
  const u32x4 x0 = *(const u32x4*)(xin + (size_t)(bn * 64 + (t >> 3)) * SL + bm * 64 + (t & 7) * 8);
  bf16x8 fa[PD][2], fb[PD][2];
#pragma unroll
  for (int it = 0; it < PD; ++it)
#pragma unroll
    for (int i = 0; i < 2; ++i) { fa[it][i] = A[(i * NS + it * 8) * 64]; fb[it][i] = B[(i * NS + it * 8) * 64]; }
  __builtin_amdgcn_sched_barrier(0);
  f32x16 acc[2][2];
#pragma unroll
  for (int i = 0; i < 2; ++i)
#pragma unroll
    for (int j = 0; j < 2; ++j)
#pragma unroll
      for (int e = 0; e < 16; ++e) acc[i][j][e] = 0.f;
#pragma unroll
  for (int it = 0; it < NIT; ++it) {
    bf16x8 ca[2], cb[2];
#pragma unroll
    for (int i = 0; i < 2; ++i) { ca[i] = fa[it % PD][i]; cb[i] = fb[it % PD][i]; }
    if (it + PD < NIT) {
#pragma unroll
      for (int i = 0; i < 2; ++i) { fa[it % PD][i] = A[(i * NS + (it + PD) * 8) * 64]; fb[it % PD][i] = B[(i * NS + (it + PD) * 8) * 64]; }
    }
    __builtin_amdgcn_sched_barrier(0);
#pragma unroll
    for (int i = 0; i < 2; ++i)
#pragma unroll
      for (int j = 0; j < 2; ++j) acc[i][j] = MFMA32(ca[i], cb[j], acc[i][j]);
    __builtin_amdgcn_sched_barrier(0);
  }
  STAMP(3, 1);
  {
    float* tl = (float*)smem + w * 64 * 68;
#pragma unroll
    for (int i = 0; i < 2; ++i)
#pragma unroll
      for (int j = 0; j < 2; ++j)
#pragma unroll
        for (int e = 0; e < 16; e += 4)
          *(float4*)(tl + (j * 32 + r) * 68 + i * 32 + 2 * e + 4 * hh) = make_float4(acc[i][j][e], acc[i][j][e + 1], acc[i][j][e + 2], acc[i][j][e + 3]);
  }
  __syncthreads();
  STAMP(3, 2);
  {
    float* t0 = (float*)smem;
    const int col = t >> 3, rq = (t & 7) * 8;
    const size_t g = (size_t)(bn * 64 + col) * SL + bm * 64 + rq;
    const u32 xv[4] = {x0[0], x0[1], x0[2], x0[3]};
    float sm[8];
#pragma unroll
    for (int k = 0; k < 8; ++k) sm[k] = 0.f;
#pragma unroll
    for (int q = 0; q < 8; ++q) {
      const float4 v0 = *(const float4*)(t0 + q * 64 * 68 + col * 68 + rq), v1 = *(const float4*)(t0 + q * 64 * 68 + col * 68 + rq + 4);
      sm[0] += v0.x; sm[1] += v0.y; sm[2] += v0.z; sm[3] += v0.w; sm[4] += v1.x; sm[5] += v1.y; sm[6] += v1.z; sm[7] += v1.w;
    }
    __syncthreads();
    u32 ow[4];
#pragma unroll
    for (int k = 0; k < 4; ++k) {
      const int i0 = (rq + 2 * k) * 65 + col, i1 = i0 + 65;
      const float a0 = rbf(bf2f(xv[k] & 0xffff) + rbf(sm[2 * k]));
      const float a1 = rbf(bf2f(xv[k] >> 16) + rbf(sm[2 * k + 1]));
      t0[2 * 64 * 65 + i0] = a0; t0[2 * 64 * 65 + i1] = a1;
      ow[k] = (u32)f2bf(a0) | ((u32)f2bf(a1) << 16);
    }
    if (!FINAL) *(uint4*)(xo + g) = make_uint4(ow[0], ow[1], ow[2], ow[3]);
  }
  __syncthreads();
  STAMP(3, 3);
  {
    const float* t2 = (const float*)smem + 2 * 64 * 65;
    const int row = t >> 3, cq = (t & 7) * 8;
    float o[8]; float s = 0.f;
#pragma unroll
    for (int k = 0; k < 8; ++k) { o[k] = t2[row * 65 + cq + k]; s += o[k]; }
    if (FINAL) {
      u32 ow[4];
#pragma unroll
      for (int k = 0; k < 4; ++k) ow[k] = (u32)f2bf(o[2 * k]) | ((u32)f2bf(o[2 * k + 1]) << 16);
      *(uint4*)(xo + (size_t)(bm * 64 + row) * DM + bn * 64 + cq) = make_uint4(ow[0], ow[1], ow[2], ow[3]);
    } else {
      s += __shfl_xor(s, 1, 64); s += __shfl_xor(s, 2, 64); s += __shfl_xor(s, 4, 64);
      const float mean = s * (1.0f / 64);
      float q = 0.f;
#pragma unroll
      for (int k = 0; k < 8; ++k) { const float dd = o[k] - mean; q += dd * dd; }
      q += __shfl_xor(q, 1, 64); q += __shfl_xor(q, 2, 64); q += __shfl_xor(q, 4, 64);
      if ((t & 7) == 0) stats[(size_t)(bm * 64 + row) * 8 + bn] = make_float2(mean, q);
    }
  }
  STAMP(3, 4);
}

extern "C" void kernel_launch(void* const* d_in, const int* in_sizes, int n_in, void* d_out, int out_size,
                              void* d_ws, size_t ws_size, hipStream_t stream) {
  const u16* x0 = (const u16*)d_in[0];
  char* ws = (char*)d_ws;
  u16* x1 = (u16*)(ws + WS_X1);
  u16* x2 = (u16*)(ws + WS_X2);
  u16* hb = (u16*)(ws + WS_H);
  u16* out = (u16*)d_out;
  hipFuncSetAttribute((const void*)conv_kernel<true>, hipFuncAttributeMaxDynamicSharedMemorySize, CONV_LDS);
  hipFuncSetAttribute((const void*)gemm1_kernel, hipFuncAttributeMaxDynamicSharedMemorySize, G1_LDS);
  hipFuncSetAttribute((const void*)gemm2_kernel<false>, hipFuncAttributeMaxDynamicSharedMemorySize, G2_LDS);
  hipFuncSetAttribute((const void*)gemm2_kernel<true>, hipFuncAttributeMaxDynamicSharedMemorySize, G2_LDS);
  prep_kernel<<<NB_PREP, PREP_T, 0, stream>>>(x0, (const u16*)d_in[1], (const u16*)d_in[2], (const u16*)d_in[3],
                                              (const u16*)d_in[4], (const u16*)d_in[5], (const u16*)d_in[6],
                                              (const u16*)d_in[7], (const u16*)d_in[10], (const u16*)d_in[11],
                                              (const u16*)d_in[12], ws);
  const u16* dskip = (const u16*)d_in[8];
  const float2* st = (const float2*)(ws + WS_ST);
  conv_kernel<true><<<DM / 2, CONV_T, CONV_LDS, stream>>>(x2, st, 1, ws + WS_KIMG, dskip, x1);
  gemm1_kernel<<<16 * 14, 512, G1_LDS, stream>>>(x1, (const u16*)(ws + WS_WGU), (const float*)(ws + WS_CS), hb);
  gemm2_kernel<false><<<32 * 8, 512, G2_LDS, stream>>>(hb, (const u16*)(ws + WS_WD), x1, x2, (float2*)(ws + WS_ST));
  conv_kernel<true><<<DM / 2, CONV_T, CONV_LDS, stream>>>(x2, st, 8, ws + WS_KIMG, dskip, x1);
  gemm1_kernel<<<16 * 14, 512, G1_LDS, stream>>>(x1, (const u16*)(ws + WS_WGU), (const float*)(ws + WS_CS), hb);
  gemm2_kernel<true><<<32 * 8, 512, G2_LDS, stream>>>(hb, (const u16*)(ws + WS_WD), x1, out, (float2*)(ws + WS_ST));
}
```
